```python
import torch

def _softmax(x, axis=-1):
    return torch.softmax(x, dim=axis)
def _gelu(x):
    return torch.nn.functional.gelu(x, approximate="tanh")
def _silu(x):
    return torch.nn.functional.silu(x)
def _sigmoid(x):
    return torch.sigmoid(x)
def _layernorm(x, axis=-1, eps=1e-5):
    m = x.mean(dim=axis, keepdim=True)
    v = x.var(dim=axis, keepdim=True, unbiased=False)
    return (x - m) / torch.sqrt(v + eps)
def _scatter_add(tgt, idx, src, axis=0):
    return tgt.clone().index_add_(axis, idx, src)
def _segment_sum(src, seg, n):
    out = torch.zeros((n,) + src.shape[1:], dtype=src.dtype)
    return out.index_add_(0, seg, src)
def _pad(x, pw):
    flat = tuple(v for pair in reversed(pw) for v in pair)
    return torch.nn.functional.pad(x, flat)

kernel_name = 'standalone__linear_kernel_rfa__prompt'

def setup_inputs():

    _w = (lambda a: (lambda t: t.to(torch.bfloat16) if t.is_floating_point() else t)(torch.as_tensor(a)))
    B, H, Sq, Sk, D, M = 1, 32, 2048, 2048, 192, 64
    return {
        "q": _w((lambda *s: torch.randn(*s, dtype=torch.bfloat16) * 0.1)(B, H, Sq, D)),
        "k": _w((lambda *s: torch.randn(*s, dtype=torch.bfloat16) * 1.6)(B, H, Sk, D)),
        "v": _w((lambda *s: torch.randn(*s, dtype=torch.bfloat16) * 1.6)(B, H, Sk, D)),
        "omega": _w((lambda *s: torch.randn(*s, dtype=torch.bfloat16) * 1.6)(M, D)),
    }

def reference(q, k, v, omega):
    d = q.shape[-1]
    m = omega.shape[0]
    scale = float(d) ** -0.25
    qs = q * scale
    ks = k * scale
    q_proj = torch.einsum("bhsd,md->bhsm", qs, omega)
    k_proj = torch.einsum("bhsd,md->bhsm", ks, omega)
    q_sq = 0.5 * torch.einsum("bhsd,bhsd->bhs", qs, qs)[..., None]
    k_sq = 0.5 * torch.einsum("bhsd,bhsd->bhs", ks, ks)[..., None]
    inv_sqrt_m = float(m) ** -0.5
    phi_q = torch.exp(q_proj - q_sq) * inv_sqrt_m + 1e-6
    phi_k = torch.exp(k_proj - k_sq) * inv_sqrt_m + 1e-6
    kv = torch.einsum("bhsm,bhsd->bhmd", phi_k, v)
    z  = torch.einsum("bhsm->bhm", phi_k)
    num = torch.einsum("bhqm,bhmd->bhqd", phi_q, kv)
    den = torch.einsum("bhqm,bhm->bhq", phi_q, z)[..., None] + 1e-6
    return num / den
```

```cpp
#include <hip/hip_runtime.h>
#include <hip/hip_bf16.h>
#include <stdint.h>
#include <math.h>

typedef __attribute__((ext_vector_type(8))) __bf16 bf16x8;
typedef __attribute__((ext_vector_type(2))) __bf16 bf16x2;
typedef __attribute__((ext_vector_type(4))) float f32x4;
typedef __attribute__((ext_vector_type(2))) float f32x2;
typedef __attribute__((ext_vector_type(4))) short s16x4;
typedef __attribute__((ext_vector_type(8))) short s16x8;
typedef __attribute__((ext_vector_type(4))) unsigned u32x4;
typedef __attribute__((ext_vector_type(2))) unsigned u32x2;

namespace rfa {

constexpr int kH = 32, kS = 2048, kD = 192, kM = 64;
constexpr int kChunk = 256;
constexpr int kNChunk = kS / kChunk;
constexpr int kRowB = kD * 2;
constexpr int kSlabFloats = 12288 + 64;
constexpr int kThreads = 512;

constexpr int kTileBytes = 32 * kRowB;
constexpr int kImgBytes = kChunk * kRowB;
constexpr int kPhiOff = kImgBytes;
constexpr int kPhiBytes = kChunk * 128;
constexpr int kOmOff1 = kPhiOff + kPhiBytes;
constexpr int kOmBytes = kM * kRowB;
constexpr int kLds1 = kOmOff1 + kOmBytes;
constexpr int kOmOff2 = kImgBytes;
constexpr int kKvOff = kOmOff2 + kOmBytes;
constexpr int kZOff = kKvOff + kD * 128;
constexpr int kLds2 = kZOff + 256;

__device__ __forceinline__ unsigned swz(unsigned row) { return (row & 2u) | ((row >> 1) & 4u); }
__device__ __forceinline__ unsigned tile_off(unsigned row, unsigned c) {
  return row * (unsigned)kRowB + ((c ^ swz(row)) << 4);
}
__device__ __forceinline__ unsigned phi_off(unsigned row, unsigned c) {
  return row * 128u + ((c ^ swz(row)) << 4);
}
__device__ __forceinline__ unsigned kvt_off(unsigned d, unsigned c) {
  return d * 128u + ((c ^ ((d >> 1) & 7u)) << 4);
}

__device__ __forceinline__ unsigned pack_bf16(float a, float b) {
  f32x2 v = {a, b};
  bf16x2 p = __builtin_convertvector(v, bf16x2);
  return __builtin_bit_cast(unsigned, p);
}
__device__ __forceinline__ float lo_f(unsigned u) { return __builtin_bit_cast(float, u << 16); }
__device__ __forceinline__ float hi_f(unsigned u) { return __builtin_bit_cast(float, u & 0xffff0000u); }
__device__ __forceinline__ float bf16_round(float x) { return lo_f(pack_bf16(x, x)); }

__device__ __forceinline__ float exp_acc(float x) {
  const float kL2E = 1.44269502162933349609375f;
  const float kL2ELo = 1.925962991e-8f;
  float th = x * kL2E;
  float tl = __builtin_fmaf(x, kL2E, -th);
  tl = __builtin_fmaf(x, kL2ELo, tl);
  float e = __builtin_amdgcn_exp2f(th);
  return e * __builtin_fmaf(tl, 0.693147180559945309f, 1.0f);
}

__device__ __forceinline__ u32x4 scale8(u32x4 x, float s) {
  u32x4 r;
#pragma unroll
  for (int j = 0; j < 4; ++j) r[j] = pack_bf16(lo_f(x[j]) * s, hi_f(x[j]) * s);
  return r;
}

__device__ __forceinline__ s16x4 tr_read(char* smem, unsigned off) {
  return __builtin_amdgcn_ds_read_tr16_b64_v4i16(
      (__attribute__((address_space(3))) s16x4*)(smem + off));
}
__device__ __forceinline__ bf16x8 join(s16x4 a, s16x4 b) {
  s16x8 v = __builtin_shufflevector(a, b, 0, 1, 2, 3, 4, 5, 6, 7);
  return __builtin_bit_cast(bf16x8, v);
}

__device__ __forceinline__ void stage_a(char* smem, unsigned tile_base, int rb, int lane,
                                        const bf16x8 (&om)[4][6], u32x2 (&phi)[4]) {
  f32x4 acc[4];
#pragma unroll
  for (int mt = 0; mt < 4; ++mt) acc[mt] = (f32x4){0.f, 0.f, 0.f, 0.f};
  float sq = 0.f;
  const unsigned row = 16 * rb + (lane & 15);
#pragma unroll
  for (int t = 0; t < 6; ++t) {
    u32x4 x = *(const u32x4*)(smem + tile_base + tile_off(row, 4 * t + (lane >> 4)));
#pragma unroll
    for (int j = 0; j < 4; ++j) {
      float a = lo_f(x[j]), b = hi_f(x[j]);
      sq = __builtin_fmaf(a, a, sq);
      sq = __builtin_fmaf(b, b, sq);
    }
    bf16x8 bfr = __builtin_bit_cast(bf16x8, x);
#pragma unroll
    for (int mt = 0; mt < 4; ++mt)
      acc[mt] = __builtin_amdgcn_mfma_f32_16x16x32_bf16(om[mt][t], bfr, acc[mt], 0, 0, 0);
  }
  sq += __shfl_xor(sq, 16);
  sq += __shfl_xor(sq, 32);
  const float hsq = 0.5f * bf16_round(sq);
#pragma unroll
  for (int mt = 0; mt < 4; ++mt) {
#pragma unroll
    for (int hp = 0; hp < 2; ++hp) {
      unsigned pj = pack_bf16(acc[mt][2 * hp], acc[mt][2 * hp + 1]);
      unsigned df = pack_bf16(lo_f(pj) - hsq, hi_f(pj) - hsq);
      unsigned ex = pack_bf16(exp_acc(lo_f(df)), exp_acc(hi_f(df)));
      phi[mt][hp] = pack_bf16(__builtin_fmaf(lo_f(ex), 0.125f, 1e-6f),
                              __builtin_fmaf(hi_f(ex), 0.125f, 1e-6f));
    }
  }
}

__device__ __forceinline__ void block_map(int b, int& head, int& chunk) {
  const int xcd = b & 7, idx = b >> 3;
  head = xcd * 4 + (idx >> 3);
  chunk = idx & 7;
}

__global__ __launch_bounds__(kThreads, 2) void rfa_kv_kernel(
    const unsigned short* __restrict__ k, const unsigned short* __restrict__ v,
    const unsigned short* __restrict__ omega, float* __restrict__ slabs, float scale) {
  __shared__ __attribute__((aligned(16))) char smem[kLds1];
  const int tid = threadIdx.x, lane = tid & 63, w = tid >> 6;
  int head, chunk;
  block_map(blockIdx.x, head, chunk);
  const size_t row0 = (size_t)head * kS + chunk * kChunk + w * 32;
  const char* kbase = (const char*)k + row0 * kRowB;
  const char* vbase = (const char*)v + row0 * kRowB;

  u32x4 oreg[3], kreg[12], vreg[12];
#pragma unroll
  for (int j = 0; j < 3; ++j) oreg[j] = *(const u32x4*)((const char*)omega + 16 * (tid + 512 * j));
#pragma unroll
  for (int i = 0; i < 12; ++i) kreg[i] = *(const u32x4*)(kbase + 16 * (64 * i + lane));
#pragma unroll
  for (int i = 0; i < 12; ++i) vreg[i] = *(const u32x4*)(vbase + 16 * (64 * i + lane));

#pragma unroll
  for (int j = 0; j < 3; ++j) {
    unsigned ci = tid + 512 * j;
    *(u32x4*)(smem + kOmOff1 + tile_off(ci / 24, ci % 24)) = oreg[j];
  }
  __syncthreads();
  bf16x8 om[4][6];
#pragma unroll
  for (int mt = 0; mt < 4; ++mt)
#pragma unroll
    for (int t = 0; t < 6; ++t)
      om[mt][t] = *(const bf16x8*)(smem + kOmOff1 + tile_off(16 * mt + (lane & 15), 4 * t + (lane >> 4)));

  const unsigned tbase = w * kTileBytes;
  unsigned saddr[12];
#pragma unroll
  for (int i = 0; i < 12; ++i) {
    unsigned ci = 64 * i + lane;
    saddr[i] = tbase + tile_off(ci / 24, ci % 24);
    *(u32x4*)(smem + saddr[i]) = scale8(kreg[i], scale);
  }
  const int g = lane >> 4, li = lane & 15;
#pragma unroll
  for (int rb = 0; rb < 2; ++rb) {
    u32x2 phi[4];
    stage_a(smem, tbase, rb, lane, om, phi);
    const unsigned prow = 32 * w + 16 * rb + li;
#pragma unroll
    for (int mt = 0; mt < 4; ++mt)
      *(u32x2*)(smem + kPhiOff + phi_off(prow, 2 * mt + (g >> 1)) + 8 * (g & 1)) = phi[mt];
  }
#pragma unroll
  for (int i = 0; i < 12; ++i) *(u32x4*)(smem + saddr[i]) = vreg[i];
  __syncthreads();

  const int mp = w & 1, dq = w >> 1;
  const int q4 = li >> 2, p4 = li & 3;
  f32x4 acc[2][3], accz[2];
#pragma unroll
  for (int mi = 0; mi < 2; ++mi) {
    accz[mi] = (f32x4){0.f, 0.f, 0.f, 0.f};
#pragma unroll
    for (int di = 0; di < 3; ++di) acc[mi][di] = (f32x4){0.f, 0.f, 0.f, 0.f};
  }
  const unsigned one2 = 0x3f803f80u;
  const bf16x8 ones = __builtin_bit_cast(bf16x8, (u32x4){one2, one2, one2, one2});
#pragma unroll
  for (int kb = 0; kb < 8; ++kb) {
    const unsigned r0 = 32 * kb + 8 * g + q4;
    bf16x8 afr[2], bfr[3];
#pragma unroll
    for (int mi = 0; mi < 2; ++mi) {
      const unsigned c = 2 * (2 * mp + mi) + (p4 >> 1);
      afr[mi] = join(tr_read(smem, kPhiOff + phi_off(r0, c) + 8 * (p4 & 1)),
                     tr_read(smem, kPhiOff + phi_off(r0 + 4, c) + 8 * (p4 & 1)));
    }
#pragma unroll
    for (int di = 0; di < 3; ++di) {
      const unsigned c = 2 * (3 * dq + di) + (p4 >> 1);
      bfr[di] = join(tr_read(smem, tile_off(r0, c) + 8 * (p4 & 1)),
                     tr_read(smem, tile_off(r0 + 4, c) + 8 * (p4 & 1)));
    }
#pragma unroll
    for (int mi = 0; mi < 2; ++mi) {
#pragma unroll
      for (int di = 0; di < 3; ++di)
        acc[mi][di] = __builtin_amdgcn_mfma_f32_16x16x32_bf16(afr[mi], bfr[di], acc[mi][di], 0, 0, 0);
      if (dq == 0) accz[mi] = __builtin_amdgcn_mfma_f32_16x16x32_bf16(afr[mi], ones, accz[mi], 0, 0, 0);
    }
  }
  float* slab = slabs + (size_t)(head * kNChunk + chunk) * kSlabFloats;
#pragma unroll
  for (int mi = 0; mi < 2; ++mi)
#pragma unroll
    for (int di = 0; di < 3; ++di)
      *(f32x4*)(slab + 4 * ((w * 6 + mi * 3 + di) * 64 + lane)) = acc[mi][di];
  if (dq == 0 && li == 0) {
#pragma unroll
    for (int mi = 0; mi < 2; ++mi) *(f32x4*)(slab + 12288 + 16 * (2 * mp + mi) + 4 * g) = accz[mi];
  }
}

__global__ __launch_bounds__(kThreads, 2) void rfa_out_kernel(
    const unsigned short* __restrict__ q, const unsigned short* __restrict__ omega,
    const float* __restrict__ slabs, unsigned short* __restrict__ out, float scale) {
  __shared__ __attribute__((aligned(16))) char smem[kLds2];
  const int tid = threadIdx.x, lane = tid & 63, w = tid >> 6;
  const int g = lane >> 4, li = lane & 15;
  int head, chunk;
  block_map(blockIdx.x, head, chunk);
  const size_t row0 = (size_t)head * kS + chunk * kChunk + w * 32;
  const char* qbase = (const char*)q + row0 * kRowB;
  char* obase = (char*)out + row0 * kRowB;

  u32x4 oreg[3], qreg[12];
#pragma unroll
  for (int j = 0; j < 3; ++j) oreg[j] = *(const u32x4*)((const char*)omega + 16 * (tid + 512 * j));
#pragma unroll
  for (int i = 0; i < 12; ++i) qreg[i] = *(const u32x4*)(qbase + 16 * (64 * i + lane));

  const float* hslab = slabs + (size_t)head * kNChunk * kSlabFloats;
#pragma unroll
  for (int u = 0; u < 6; ++u) {
    f32x4 s = *(const f32x4*)(hslab + 4 * (u * 512 + tid));
#pragma unroll
    for (int p = 1; p < kNChunk; ++p)
      s += *(const f32x4*)(hslab + (size_t)p * kSlabFloats + 4 * (u * 512 + tid));
    const int T = 8 * u + w;
    const int wp = T / 6, ti = T % 6;
    const int mi = ti / 3, di = ti % 3;
    const int mt = 2 * (wp & 1) + mi;
    const unsigned d = 16 * (3 * (wp >> 1) + di) + li;
    u32x2 pk = {pack_bf16(s[0], s[1]), pack_bf16(s[2], s[3])};
    *(u32x2*)(smem + kKvOff + kvt_off(d, (mt >> 1) * 4 + g) + 8 * (mt & 1)) = pk;
  }
  if (tid < 16) {
    f32x4 s = *(const f32x4*)(hslab + 12288 + 4 * tid);
#pragma unroll
    for (int p = 1; p < kNChunk; ++p)
      s += *(const f32x4*)(hslab + (size_t)p * kSlabFloats + 12288 + 4 * tid);
    f32x4 zr = {bf16_round(s[0]), bf16_round(s[1]), bf16_round(s[2]), bf16_round(s[3])};
    *(f32x4*)(smem + kZOff + 16 * tid) = zr;
  }
#pragma unroll
  for (int j = 0; j < 3; ++j) {
    unsigned ci = tid + 512 * j;
    *(u32x4*)(smem + kOmOff2 + tile_off(ci / 24, ci % 24)) = oreg[j];
  }
  __syncthreads();

  bf16x8 om[4][6];
#pragma unroll
  for (int mt = 0; mt < 4; ++mt)
#pragma unroll
    for (int t = 0; t < 6; ++t)
      om[mt][t] = *(const bf16x8*)(smem + kOmOff2 + tile_off(16 * mt + li, 4 * t + g));

  const unsigned tbase = w * kTileBytes;
  unsigned saddr[12];
#pragma unroll
  for (int i = 0; i < 12; ++i) {
    unsigned ci = 64 * i + lane;
    saddr[i] = tbase + tile_off(ci / 24, ci % 24);
    *(u32x4*)(smem + saddr[i]) = scale8(qreg[i], scale);
  }
  f32x4 zf[4];
#pragma unroll
  for (int mt = 0; mt < 4; ++mt) zf[mt] = *(const f32x4*)(smem + kZOff + 4 * (16 * mt + 4 * g));

  bf16x8 bq[2][2];
  float rinv[2];
#pragma unroll
  for (int rb = 0; rb < 2; ++rb) {
    u32x2 phi[4];
    stage_a(smem, tbase, rb, lane, om, phi);
    float dn = 0.f;
#pragma unroll
    for (int mt = 0; mt < 4; ++mt) {
      dn = __builtin_fmaf(lo_f(phi[mt][0]), zf[mt][0], dn);
      dn = __builtin_fmaf(hi_f(phi[mt][0]), zf[mt][1], dn);
      dn = __builtin_fmaf(lo_f(phi[mt][1]), zf[mt][2], dn);
      dn = __builtin_fmaf(hi_f(phi[mt][1]), zf[mt][3], dn);
    }
    dn += __shfl_xor(dn, 16);
    dn += __shfl_xor(dn, 32);
    const float den = bf16_round(bf16_round(dn) + 1e-6f);
    rinv[rb] = 1.0f / den;
#pragma unroll
    for (int u2 = 0; u2 < 2; ++u2) {
      u32x4 b4 = {phi[2 * u2][0], phi[2 * u2][1], phi[2 * u2 + 1][0], phi[2 * u2 + 1][1]};
      bq[rb][u2] = __builtin_bit_cast(bf16x8, b4);
    }
  }

#pragma unroll
  for (int T = 0; T < 12; ++T) {
    const unsigned d = 16 * T + li;
    bf16x8 a0 = *(const bf16x8*)(smem + kKvOff + kvt_off(d, g));
    bf16x8 a1 = *(const bf16x8*)(smem + kKvOff + kvt_off(d, 4 + g));
#pragma unroll
    for (int rb = 0; rb < 2; ++rb) {
      f32x4 acc = __builtin_amdgcn_mfma_f32_16x16x32_bf16(a0, bq[rb][0], (f32x4){0.f, 0.f, 0.f, 0.f}, 0, 0, 0);
      acc = __builtin_amdgcn_mfma_f32_16x16x32_bf16(a1, bq[rb][1], acc, 0, 0, 0);
      unsigned n01 = pack_bf16(acc[0], acc[1]), n23 = pack_bf16(acc[2], acc[3]);
      const float r = rinv[rb];
      u32x2 o = {pack_bf16(lo_f(n01) * r, hi_f(n01) * r), pack_bf16(lo_f(n23) * r, hi_f(n23) * r)};
      *(u32x2*)(smem + tbase + tile_off(16 * rb + li, 2 * T + (g >> 1)) + 8 * (g & 1)) = o;
    }
  }
#pragma unroll
  for (int i = 0; i < 12; ++i)
    *(u32x4*)(obase + 16 * (64 * i + lane)) = *(const u32x4*)(smem + saddr[i]);
}

}

extern "C" void kernel_launch(void* const* d_in, const int* in_sizes, int n_in, void* d_out,
                              int out_size, void* d_ws, size_t ws_size, hipStream_t stream) {
  (void)in_sizes; (void)n_in; (void)out_size; (void)ws_size;
  const unsigned short* q = (const unsigned short*)d_in[0];
  const unsigned short* k = (const unsigned short*)d_in[1];
  const unsigned short* v = (const unsigned short*)d_in[2];
  const unsigned short* omega = (const unsigned short*)d_in[3];
  unsigned short* out = (unsigned short*)d_out;
  float* slabs = (float*)d_ws;
  const float scale = (float)pow((double)rfa::kD, -0.25);
  const int grid = rfa::kH * rfa::kNChunk;
  rfa::rfa_kv_kernel<<<grid, rfa::kThreads, 0, stream>>>(k, v, omega, slabs, scale);
  rfa::rfa_out_kernel<<<grid, rfa::kThreads, 0, stream>>>(q, omega, slabs, out, scale);
}
```

```cpp
#include <hip/hip_runtime.h>
#include <hip/hip_bf16.h>
#include <stdint.h>
#include <math.h>

typedef __attribute__((ext_vector_type(8))) __bf16 bf16x8;
typedef __attribute__((ext_vector_type(2))) __bf16 bf16x2;
typedef __attribute__((ext_vector_type(4))) float f32x4;
typedef __attribute__((ext_vector_type(2))) float f32x2;
typedef __attribute__((ext_vector_type(4))) short s16x4;
typedef __attribute__((ext_vector_type(8))) short s16x8;
typedef __attribute__((ext_vector_type(4))) unsigned u32x4;
typedef __attribute__((ext_vector_type(2))) unsigned u32x2;

namespace rfa {

constexpr int kH = 32, kS = 2048, kD = 192, kM = 64;
constexpr int kChunk = 256;
constexpr int kNChunk = kS / kChunk;
constexpr int kRowB = kD * 2;
constexpr int kSlabFloats = 12288 + 64;
constexpr int kThreads = 512;

constexpr int kTS = 400;
constexpr int kPS = 136;
constexpr int kKS = 144;
constexpr int kTileBytes = 32 * kTS;
constexpr int kImgBytes = kChunk * kTS;
constexpr int kPhiOff = kImgBytes;
constexpr int kOmOff1 = kPhiOff + kChunk * kPS;
constexpr int kOmBytes = kM * kTS;
constexpr int kLds1 = kOmOff1 + kOmBytes;
constexpr int kOmOff2 = kImgBytes;
constexpr int kKvOff = kOmOff2 + kOmBytes;
constexpr int kZOff = kKvOff + kD * kKS;
constexpr int kLds2 = kZOff + 256;

__device__ __forceinline__ unsigned pack_bf16(float a, float b) {
  f32x2 v = {a, b};
  bf16x2 p = __builtin_convertvector(v, bf16x2);
  return __builtin_bit_cast(unsigned, p);
}
__device__ __forceinline__ float lo_f(unsigned u) { return __builtin_bit_cast(float, u << 16); }
__device__ __forceinline__ float hi_f(unsigned u) { return __builtin_bit_cast(float, u & 0xffff0000u); }
__device__ __forceinline__ float bf16_round(float x) { return lo_f(pack_bf16(x, x)); }

__device__ __forceinline__ float exp_acc(float x) {
  const float kL2E = 1.44269502162933349609375f;
  const float kL2ELo = 1.925962991e-8f;
  float th = x * kL2E;
  float tl = __builtin_fmaf(x, kL2E, -th);
  tl = __builtin_fmaf(x, kL2ELo, tl);
  float e = __builtin_amdgcn_exp2f(th);
  return e * __builtin_fmaf(tl, 0.693147180559945309f, 1.0f);
}

__device__ __forceinline__ u32x4 ld_nt(const void* p) { return __builtin_nontemporal_load((const u32x4*)p); }
__device__ __forceinline__ void st_nt(void* p, u32x4 v) { __builtin_nontemporal_store(v, (u32x4*)p); }

__device__ __forceinline__ u32x4 scale8(u32x4 x, float s) {
  u32x4 r;
#pragma unroll
  for (int j = 0; j < 4; ++j) r[j] = pack_bf16(lo_f(x[j]) * s, hi_f(x[j]) * s);
  return r;
}

__device__ __forceinline__ s16x4 tr_read(char* smem, unsigned off) {
  return __builtin_amdgcn_ds_read_tr16_b64_v4i16(
      (__attribute__((address_space(3))) s16x4*)(smem + off));
}
__device__ __forceinline__ bf16x8 join(s16x4 a, s16x4 b) {
  s16x8 v = __builtin_shufflevector(a, b, 0, 1, 2, 3, 4, 5, 6, 7);
  return __builtin_bit_cast(bf16x8, v);
}

__device__ __forceinline__ void stage_a(char* smem, unsigned tile_base, int rb, int lane,
                                        const bf16x8 (&om)[4][6], u32x2 (&phi)[4]) {
  f32x4 acc[4], accn = (f32x4){0.f, 0.f, 0.f, 0.f};
#pragma unroll
  for (int mt = 0; mt < 4; ++mt) acc[mt] = (f32x4){0.f, 0.f, 0.f, 0.f};
  const unsigned fa = tile_base + (16 * rb + (lane & 15)) * kTS + 16 * (lane >> 4);
#pragma unroll
  for (int t = 0; t < 6; ++t) {
    bf16x8 bfr = *(const bf16x8*)(smem + fa + 64 * t);
    accn = __builtin_amdgcn_mfma_f32_16x16x32_bf16(bfr, bfr, accn, 0, 0, 0);
#pragma unroll
    for (int mt = 0; mt < 4; ++mt)
      acc[mt] = __builtin_amdgcn_mfma_f32_16x16x32_bf16(om[mt][t], bfr, acc[mt], 0, 0, 0);
  }
  const int rsel = lane & 3;
  float dg = rsel == 0 ? accn[0] : (rsel == 1 ? accn[1] : (rsel == 2 ? accn[2] : accn[3]));
  const float sq = __shfl(dg, (lane & 15) + 4 * (lane & 12));
  const float hsq = 0.5f * bf16_round(sq);
#pragma unroll
  for (int mt = 0; mt < 4; ++mt) {
#pragma unroll
    for (int hp = 0; hp < 2; ++hp) {
      unsigned pj = pack_bf16(acc[mt][2 * hp], acc[mt][2 * hp + 1]);
      unsigned df = pack_bf16(lo_f(pj) - hsq, hi_f(pj) - hsq);
      unsigned ex = pack_bf16(exp_acc(lo_f(df)), exp_acc(hi_f(df)));
      phi[mt][hp] = pack_bf16(__builtin_fmaf(lo_f(ex), 0.125f, 1e-6f),
                              __builtin_fmaf(hi_f(ex), 0.125f, 1e-6f));
    }
  }
}

__device__ __forceinline__ void block_map(int b, int& head, int& chunk) {
  const int xcd = b & 7, idx = b >> 3;
  head = xcd * 4 + (idx >> 3);
  chunk = idx & 7;
}

__device__ __forceinline__ unsigned stage_off(unsigned P) { return (P / 24) * kTS + (P % 24) * 16; }

__global__ __launch_bounds__(kThreads, 2) void rfa_kv_kernel(
    const unsigned short* __restrict__ k, const unsigned short* __restrict__ v,
    const unsigned short* __restrict__ omega, float* __restrict__ slabs, float scale) {
  __shared__ __attribute__((aligned(16))) char smem[kLds1];
  const int tid = threadIdx.x, lane = tid & 63, w = tid >> 6;
  const int g = lane >> 4, li = lane & 15;
  int head, chunk;
  block_map(blockIdx.x, head, chunk);
  const size_t row0 = (size_t)head * kS + chunk * kChunk + w * 32;
  const char* kbase = (const char*)k + row0 * kRowB + 16 * lane;
  const char* vbase = (const char*)v + row0 * kRowB + 16 * lane;

  u32x4 oreg[3], treg[12];
#pragma unroll
  for (int j = 0; j < 3; ++j) oreg[j] = *(const u32x4*)((const char*)omega + 16 * (tid + 512 * j));
#pragma unroll
  for (int i = 0; i < 12; ++i) treg[i] = ld_nt(kbase + 1024 * i);

#pragma unroll
  for (int j = 0; j < 3; ++j) *(u32x4*)(smem + kOmOff1 + stage_off(tid + 512 * j)) = oreg[j];
  __syncthreads();
  bf16x8 om[4][6];
#pragma unroll
  for (int mt = 0; mt < 4; ++mt)
#pragma unroll
    for (int t = 0; t < 6; ++t)
      om[mt][t] = *(const bf16x8*)(smem + kOmOff1 + (16 * mt + li) * kTS + 16 * (4 * t + g));

  const unsigned tbase = w * kTileBytes;
  unsigned sb[3];
#pragma unroll
  for (int b = 0; b < 3; ++b) sb[b] = tbase + stage_off(64 * b + lane);
#pragma unroll
  for (int i = 0; i < 12; ++i) {
    *(u32x4*)(smem + sb[i % 3] + 8 * kTS * (i / 3)) = scale8(treg[i], scale);
    __builtin_amdgcn_sched_barrier(0);
    treg[i] = ld_nt(vbase + 1024 * i);
    __builtin_amdgcn_sched_barrier(0);
  }
#pragma unroll
  for (int rb = 0; rb < 2; ++rb) {
    u32x2 phi[4];
    stage_a(smem, tbase, rb, lane, om, phi);
    const unsigned prow = 32 * w + 16 * rb + li;
#pragma unroll
    for (int mt = 0; mt < 4; ++mt)
      *(u32x2*)(smem + kPhiOff + prow * kPS + 32 * mt + 8 * g) = phi[mt];
  }
#pragma unroll
  for (int i = 0; i < 12; ++i) *(u32x4*)(smem + sb[i % 3] + 8 * kTS * (i / 3)) = treg[i];
  __syncthreads();

  const int mp = w & 1, dq = w >> 1;
  const int q4 = li >> 2, p4 = li & 3;
  f32x4 acc[2][3], accz[2];
#pragma unroll
  for (int mi = 0; mi < 2; ++mi) {
    accz[mi] = (f32x4){0.f, 0.f, 0.f, 0.f};
#pragma unroll
    for (int di = 0; di < 3; ++di) acc[mi][di] = (f32x4){0.f, 0.f, 0.f, 0.f};
  }
  const unsigned one2 = 0x3f803f80u;
  const bf16x8 ones = __builtin_bit_cast(bf16x8, (u32x4){one2, one2, one2, one2});
  const unsigned pa = kPhiOff + (8 * g + q4) * kPS + 64 * mp + 8 * p4;
  const unsigned va = (8 * g + q4) * kTS + 96 * dq + 8 * p4;
#pragma unroll
  for (int kb = 0; kb < 8; ++kb) {
    bf16x8 afr[2], bfr[3];
#pragma unroll
    for (int mi = 0; mi < 2; ++mi)
      afr[mi] = join(tr_read(smem, pa + 32 * kb * kPS + 32 * mi),
                     tr_read(smem, pa + (32 * kb + 4) * kPS + 32 * mi));
#pragma unroll
    for (int di = 0; di < 3; ++di)
      bfr[di] = join(tr_read(smem, va + 32 * kb * kTS + 32 * di),
                     tr_read(smem, va + (32 * kb + 4) * kTS + 32 * di));
#pragma unroll
    for (int mi = 0; mi < 2; ++mi) {
#pragma unroll
      for (int di = 0; di < 3; ++di)
        acc[mi][di] = __builtin_amdgcn_mfma_f32_16x16x32_bf16(afr[mi], bfr[di], acc[mi][di], 0, 0, 0);
      if (dq == 0) accz[mi] = __builtin_amdgcn_mfma_f32_16x16x32_bf16(afr[mi], ones, accz[mi], 0, 0, 0);
    }
  }
  float* slab = slabs + (size_t)(head * kNChunk + chunk) * kSlabFloats;
#pragma unroll
  for (int mi = 0; mi < 2; ++mi)
#pragma unroll
    for (int di = 0; di < 3; ++di)
      *(f32x4*)(slab + 4 * ((w * 6 + mi * 3 + di) * 64 + lane)) = acc[mi][di];
  if (dq == 0 && li == 0) {
#pragma unroll
    for (int mi = 0; mi < 2; ++mi) *(f32x4*)(slab + 12288 + 16 * (2 * mp + mi) + 4 * g) = accz[mi];
  }
}

__global__ __launch_bounds__(kThreads, 2) void rfa_out_kernel(
    const unsigned short* __restrict__ q, const unsigned short* __restrict__ omega,
    const float* __restrict__ slabs, unsigned short* __restrict__ out, float scale) {
  __shared__ __attribute__((aligned(16))) char smem[kLds2];
  const int tid = threadIdx.x, lane = tid & 63, w = tid >> 6;
  const int g = lane >> 4, li = lane & 15;
  int head, chunk;
  block_map(blockIdx.x, head, chunk);
  const size_t row0 = (size_t)head * kS + chunk * kChunk + w * 32;
  const char* qbase = (const char*)q + row0 * kRowB + 16 * lane;
  char* obase = (char*)out + row0 * kRowB + 16 * lane;

  u32x4 oreg[3], treg[12];
#pragma unroll
  for (int j = 0; j < 3; ++j) oreg[j] = *(const u32x4*)((const char*)omega + 16 * (tid + 512 * j));
#pragma unroll
  for (int i = 0; i < 12; ++i) treg[i] = ld_nt(qbase + 1024 * i);
#pragma unroll
  for (int j = 0; j < 3; ++j) *(u32x4*)(smem + kOmOff2 + stage_off(tid + 512 * j)) = oreg[j];

  const unsigned tbase = w * kTileBytes;
  unsigned sb[3];
#pragma unroll
  for (int b = 0; b < 3; ++b) sb[b] = tbase + stage_off(64 * b + lane);
#pragma unroll
  for (int i = 0; i < 12; ++i)
    *(u32x4*)(smem + sb[i % 3] + 8 * kTS * (i / 3)) = scale8(treg[i], scale);

  const float* hslab = slabs + (size_t)head * kNChunk * kSlabFloats;
#pragma unroll
  for (int u = 0; u < 6; ++u) {
    f32x4 s = *(const f32x4*)(hslab + 4 * (u * 512 + tid));
#pragma unroll
    for (int p = 1; p < kNChunk; ++p)
      s += *(const f32x4*)(hslab + (size_t)p * kSlabFloats + 4 * (u * 512 + tid));
    const int T = 8 * u + w;
    const int wp = T / 6, ti = T % 6;
    const int mi = ti / 3, di = ti % 3;
    const int mt = 2 * (wp & 1) + mi;
    const unsigned d = 16 * (3 * (wp >> 1) + di) + li;
    u32x2 pk = {pack_bf16(s[0], s[1]), pack_bf16(s[2], s[3])};
    *(u32x2*)(smem + kKvOff + d * kKS + 64 * (mt >> 1) + 16 * g + 8 * (mt & 1)) = pk;
  }
  if (tid < 16) {
    f32x4 s = *(const f32x4*)(hslab + 12288 + 4 * tid);
#pragma unroll
    for (int p = 1; p < kNChunk; ++p)
      s += *(const f32x4*)(hslab + (size_t)p * kSlabFloats + 12288 + 4 * tid);
    f32x4 zr = {bf16_round(s[0]), bf16_round(s[1]), bf16_round(s[2]), bf16_round(s[3])};
    *(f32x4*)(smem + kZOff + 16 * tid) = zr;
  }
  __syncthreads();

  bf16x8 om[4][6];
#pragma unroll
  for (int mt = 0; mt < 4; ++mt)
#pragma unroll
    for (int t = 0; t < 6; ++t)
      om[mt][t] = *(const bf16x8*)(smem + kOmOff2 + (16 * mt + li) * kTS + 16 * (4 * t + g));
  f32x4 zf[4];
#pragma unroll
  for (int mt = 0; mt < 4; ++mt) zf[mt] = *(const f32x4*)(smem + kZOff + 4 * (16 * mt + 4 * g));

  bf16x8 bq[2][2];
  float rinv[2];
#pragma unroll
  for (int rb = 0; rb < 2; ++rb) {
    u32x2 phi[4];
    stage_a(smem, tbase, rb, lane, om, phi);
    float dn = 0.f;
#pragma unroll
    for (int mt = 0; mt < 4; ++mt) {
      dn = __builtin_fmaf(lo_f(phi[mt][0]), zf[mt][0], dn);
      dn = __builtin_fmaf(hi_f(phi[mt][0]), zf[mt][1], dn);
      dn = __builtin_fmaf(lo_f(phi[mt][1]), zf[mt][2], dn);
      dn = __builtin_fmaf(hi_f(phi[mt][1]), zf[mt][3], dn);
    }
    dn += __shfl_xor(dn, 16);
    dn += __shfl_xor(dn, 32);
    const float den = bf16_round(bf16_round(dn) + 1e-6f);
    rinv[rb] = 1.0f / den;
#pragma unroll
    for (int u2 = 0; u2 < 2; ++u2) {
      u32x4 b4 = {phi[2 * u2][0], phi[2 * u2][1], phi[2 * u2 + 1][0], phi[2 * u2 + 1][1]};
      bq[rb][u2] = __builtin_bit_cast(bf16x8, b4);
    }
  }

#pragma unroll
  for (int T = 0; T < 12; ++T) {
    const unsigned ka = kKvOff + (16 * T + li) * kKS + 16 * g;
    bf16x8 a0 = *(const bf16x8*)(smem + ka);
    bf16x8 a1 = *(const bf16x8*)(smem + ka + 64);
#pragma unroll
    for (int rb = 0; rb < 2; ++rb) {
      f32x4 acc = __builtin_amdgcn_mfma_f32_16x16x32_bf16(a0, bq[rb][0], (f32x4){0.f, 0.f, 0.f, 0.f}, 0, 0, 0);
      acc = __builtin_amdgcn_mfma_f32_16x16x32_bf16(a1, bq[rb][1], acc, 0, 0, 0);
      unsigned n01 = pack_bf16(acc[0], acc[1]), n23 = pack_bf16(acc[2], acc[3]);
      const float r = rinv[rb];
      u32x2 o = {pack_bf16(lo_f(n01) * r, hi_f(n01) * r), pack_bf16(lo_f(n23) * r, hi_f(n23) * r)};
      *(u32x2*)(smem + tbase + (16 * rb + li) * kTS + 32 * T + 8 * g) = o;
    }
  }
#pragma unroll
  for (int i = 0; i < 12; ++i)
    st_nt(obase + 1024 * i, *(const u32x4*)(smem + sb[i % 3] + 8 * kTS * (i / 3)));
}

}

extern "C" void kernel_launch(void* const* d_in, const int* in_sizes, int n_in, void* d_out,
                              int out_size, void* d_ws, size_t ws_size, hipStream_t stream) {
  (void)in_sizes; (void)n_in; (void)out_size; (void)ws_size;
  const unsigned short* q = (const unsigned short*)d_in[0];
  const unsigned short* k = (const unsigned short*)d_in[1];
  const unsigned short* v = (const unsigned short*)d_in[2];
  const unsigned short* omega = (const unsigned short*)d_in[3];
  unsigned short* out = (unsigned short*)d_out;
  float* slabs = (float*)d_ws;
  const float scale = (float)pow((double)rfa::kD, -0.25);
  const int grid = rfa::kH * rfa::kNChunk;
  rfa::rfa_kv_kernel<<<grid, rfa::kThreads, 0, stream>>>(k, v, omega, slabs, scale);
  rfa::rfa_out_kernel<<<grid, rfa::kThreads, 0, stream>>>(q, omega, slabs, out, scale);
}
```
